```python
import torch

def _softmax(x, axis=-1):
    return torch.softmax(x, dim=axis)

def _gelu(x):
    return torch.nn.functional.gelu(x, approximate='tanh')

def _silu(x):
    return torch.nn.functional.silu(x)

def _sigmoid(x):
    return torch.sigmoid(x)

def _layernorm(x, axis=-1, eps=1e-05):
    m = x.mean(dim=axis, keepdim=True)
    v = x.var(dim=axis, keepdim=True, unbiased=False)
    return (x - m) / torch.sqrt(v + eps)

def _scatter_add(tgt, idx, src, axis=0):
    return tgt.clone().index_add_(axis, idx, src)

def _segment_sum(src, seg, n):
    out = torch.zeros((n,) + src.shape[1:], dtype=src.dtype)
    return out.index_add_(0, seg, src)

def _pad(x, pw):
    flat = tuple((v for pair in reversed(pw) for v in pair))
    return torch.nn.functional.pad(x, flat)
kernel_name = 'adaLN_cond__compressed_kv_cla__mixer_channel_mlp__decode'

def setup_inputs():

    _w = lambda a: (lambda t: t.to(torch.bfloat16) if t.is_floating_point() else t)(torch.as_tensor(a))

    def _A_setup():
        B, H, Sq, Sk, D, M = (1, 32, 1, 4096, 128, 512)
        Dc = 4 * D
        return {'x': _w((lambda *s: torch.randn(*s, dtype=torch.bfloat16) * 0.1)(B, Sq, M)), 'c_kv': _w((lambda *s: torch.randn(*s, dtype=torch.bfloat16) * 0.8)(B, Sk, Dc)), 'w_q': _w((lambda *s: torch.randn(*s, dtype=torch.bfloat16) * 0.8)(M, H, D) * M ** (-0.5)), 'w_uk': _w((lambda *s: torch.randn(*s, dtype=torch.bfloat16) * 0.8)(Dc, H, D) * Dc ** (-0.5)), 'w_uv': _w((lambda *s: torch.randn(*s, dtype=torch.bfloat16) * 0.8)(Dc, H, D) * Dc ** (-0.5)), 'w_o': _w((lambda *s: torch.randn(*s, dtype=torch.bfloat16) * 0.8)(H, D, M) * (H * D) ** (-0.5))}
    _A_in = _A_setup()

    def _B_setup():
        B, S, D, F = (1, 1, 512, 1376)
        return {'x': _w((lambda *s: torch.randn(*s, dtype=torch.bfloat16) * 0.1)(B, S, D)), 'w1': _w((lambda *s: torch.randn(*s, dtype=torch.bfloat16) * 3.2)(D, F) * D ** (-0.5)), 'b1': _w((lambda *s: torch.randn(*s, dtype=torch.bfloat16) * 3.2)(F)), 'w2': _w((lambda *s: torch.randn(*s, dtype=torch.bfloat16) * 3.2)(F, D) * F ** (-0.5)), 'b2': _w((lambda *s: torch.randn(*s, dtype=torch.bfloat16) * 3.2)(D))}
    _B_in = _B_setup()
    return {'A_x': _A_in['x'], 'A_c_kv': _A_in['c_kv'], 'A_w_q': _A_in['w_q'], 'A_w_uk': _A_in['w_uk'], 'A_w_uv': _A_in['w_uv'], 'A_w_o': _A_in['w_o'], 'B_x': _B_in['x'], 'B_w1': _B_in['w1'], 'B_b1': _B_in['b1'], 'B_w2': _B_in['w2'], 'B_b2': _B_in['b2'], 'c': _w((lambda *s: torch.randn(*s, dtype=torch.bfloat16) * 0.4)(1, 512) * 8.0), 'w_mod': _w((lambda *s: torch.randn(*s, dtype=torch.bfloat16) * 0.4)(512, 6 * 512) * 512 ** (-0.5) * 8.0)}

def reference(A_x, A_c_kv, A_w_q, A_w_uk, A_w_uv, A_w_o, B_x, B_w1, B_b1, B_w2, B_b2, c, w_mod):

    def _A_ref(x, c_kv, w_q, w_uk, w_uv, w_o):
        B, Sq, M = (x.shape[0], x.shape[1], x.shape[2])
        Sk = c_kv.shape[1]
        H, D = (w_q.shape[1], w_q.shape[2])
        q = torch.einsum('bqm,mhd->bhqd', x, w_q)
        k = torch.einsum('bkc,chd->bhkd', c_kv, w_uk)
        v = torch.einsum('bkc,chd->bhkd', c_kv, w_uv)
        scores = torch.einsum('bhqd,bhkd->bhqk', q, k) * D ** (-0.5)
        off = Sk - Sq
        qi = torch.arange(Sq)[:, None]
        kj = torch.arange(Sk)[None, :]
        cmask = kj <= qi + off
        scores = torch.where(cmask[None, None], scores, -1000000000.0)
        p = _softmax(scores, axis=-1)
        ctx = torch.einsum('bhqk,bhkd->bhqd', p, v)
        return torch.einsum('bhqd,hdm->bqm', ctx, w_o)

    def _B_ref(x, w1, b1, w2, b2):
        y = _layernorm(x, axis=-1)
        h = _gelu(torch.einsum('bsd,df->bsf', y, w1) + b1)
        return torch.einsum('bsf,fd->bsd', h, w2) + b2
    x = A_x
    mod = (_silu(c) @ w_mod).reshape(c.shape[0], 6, 1, -1)
    sa, ca, ga, sb, cb, gb = (mod[:, 0].clone(), mod[:, 1].clone(), mod[:, 2].clone(), mod[:, 3].clone(), mod[:, 4].clone(), mod[:, 5].clone())
    h = _layernorm(x, axis=-1) * (1 + ca) + sa
    x = x + ga * _A_ref(h, A_c_kv, A_w_q, A_w_uk, A_w_uv, A_w_o)
    h = _layernorm(x, axis=-1) * (1 + cb) + sb
    x = x + gb * _B_ref(h, B_w1, B_b1, B_w2, B_b2)
    return x
```

```cpp
#include <hip/hip_runtime.h>
#include <hip/hip_bf16.h>

typedef unsigned short bf16_t;
typedef unsigned long long u64;
typedef unsigned v4u __attribute__((ext_vector_type(4)));
typedef unsigned v2u __attribute__((ext_vector_type(2)));
typedef float f32x4 __attribute__((ext_vector_type(4)));
typedef short bf16x8 __attribute__((ext_vector_type(8)));
typedef short s16x4 __attribute__((ext_vector_type(4)));
#define LAS __attribute__((address_space(3)))
#define GAS __attribute__((address_space(1)))

constexpr int NWG = 256, NT = 512;
constexpr int AUX_SC1 = 16;
constexpr unsigned SENT = 0xFFFFFFFFu;
constexpr unsigned SPIN_CAP = 1u << 20;

constexpr int OFF_EPOCH = 0;
constexpr int OFF_QA = 16384;
constexpr int OFF_STAT = OFF_QA + 65536;
constexpr int OFF_CTXP = OFF_STAT + 16384;
constexpr int OFF_AOP = OFF_CTXP + 32768;
constexpr int OFF_MOD = OFF_AOP + 16384;
constexpr int OFF_QP = OFF_MOD + 16384;
constexpr int OFF_G1 = OFF_QP + 65536;
constexpr int OFF_OUTP = OFF_G1 + 32768;
constexpr int OFF_CLR = OFF_OUTP + 43 * 2048;
constexpr int OFF_PART = 524288;
constexpr int WS_BYTES = OFF_PART + 32 * 4 * 8 * 1024;
static_assert(OFF_CLR + 32768 <= OFF_PART, "ws");

constexpr int KV_STRIDE = 1024 + 32;
constexpr int L_KV = 0;
constexpr int P_STRIDE = 256 + 16;
constexpr int L_P = 128 * KV_STRIDE;
constexpr int L_RED = L_P + 16 * P_STRIDE;
constexpr int L_RED2 = L_RED + 2048;
constexpr int LDS_BYTES = L_RED2 + 2048;
static_assert(LDS_BYTES <= 160 * 1024, "lds");
constexpr int TR_STRIDE = 64 + 16;
constexpr int TR_WAVE = 16384;

struct Params {
  const bf16_t *x, *ckv, *wq, *wuk, *wuv, *wo, *w1, *b1, *w2, *b2, *c, *wmod;
  bf16_t* out;
  unsigned char* ws;
};

__device__ __forceinline__ float asf(unsigned u) { return __builtin_bit_cast(float, u); }
__device__ __forceinline__ unsigned asu(float f) { return __builtin_bit_cast(unsigned, f); }
__device__ __forceinline__ float bf2f(unsigned b) { return asf(b << 16); }
__device__ __forceinline__ float bflo(unsigned w) { return asf(w << 16); }
__device__ __forceinline__ float bfhi(unsigned w) { return asf(w & 0xFFFF0000u); }
__device__ __forceinline__ unsigned f2bf(float f) { return (unsigned)__builtin_bit_cast(unsigned short, (__bf16)f); }
__device__ __forceinline__ float rbf(float f) { return (float)(__bf16)f; }
__device__ __forceinline__ unsigned san(unsigned u) { return u < 0xFFFFFFFEu ? u : 0xFFFFFFFEu; }
__device__ __forceinline__ unsigned sanf(float f) { return san(asu(f)); }

typedef __amdgpu_buffer_rsrc_t rsrc_t;
__device__ __forceinline__ v4u ld128(rsrc_t rs, int off) { return __builtin_amdgcn_raw_buffer_load_b128(rs, off, 0, AUX_SC1); }
__device__ __forceinline__ v2u ld64(rsrc_t rs, int off) { return __builtin_amdgcn_raw_buffer_load_b64(rs, off, 0, AUX_SC1); }
__device__ __forceinline__ unsigned ld32(rsrc_t rs, int off) { return __builtin_amdgcn_raw_buffer_load_b32(rs, off, 0, AUX_SC1); }
__device__ __forceinline__ void st128(rsrc_t rs, int off, v4u v) { __builtin_amdgcn_raw_buffer_store_b128(v, rs, off, 0, AUX_SC1); }
__device__ __forceinline__ void st64(rsrc_t rs, int off, v2u v) { __builtin_amdgcn_raw_buffer_store_b64(v, rs, off, 0, AUX_SC1); }
__device__ __forceinline__ void st32(rsrc_t rs, int off, unsigned v) { __builtin_amdgcn_raw_buffer_store_b32(v, rs, off, 0, AUX_SC1); }
#define MEMBAR() asm volatile("" ::: "memory")
#define POLL _Pragma("unroll 1") for (unsigned sp = 0; sp < SPIN_CAP; ++sp)

__device__ __forceinline__ v4u ldw(const bf16_t* p) { return __builtin_nontemporal_load((const GAS v4u*)p); }

__device__ __forceinline__ f32x4 mfma16(v4u a, v4u b, f32x4 c) {
  return __builtin_amdgcn_mfma_f32_16x16x32_bf16(__builtin_bit_cast(bf16x8, a), __builtin_bit_cast(bf16x8, b), c, 0, 0, 0);
}
template <int CTRL> __device__ __forceinline__ unsigned dppu(unsigned v) {
  return (unsigned)__builtin_amdgcn_update_dpp(0, (int)v, CTRL, 0xf, 0xf, true);
}
template <int CTRL> __device__ __forceinline__ float dpp(float v) { return asf(dppu<CTRL>(asu(v))); }
__device__ __forceinline__ float row_sum(float v) {
  v += dpp<0xB1>(v); v += dpp<0x4E>(v); v += dpp<0x141>(v); v += dpp<0x140>(v); return v;
}
__device__ __forceinline__ float row_max(float v) {
  v = fmaxf(v, dpp<0xB1>(v)); v = fmaxf(v, dpp<0x4E>(v)); v = fmaxf(v, dpp<0x141>(v)); v = fmaxf(v, dpp<0x140>(v)); return v;
}
__device__ __forceinline__ float swap32_add(float a, float b) {
  auto r = __builtin_amdgcn_permlane32_swap(asu(a), asu(b), false, false);
  const unsigned x = r[0], y = r[1];
  return asf(x) + asf(y);
}
__device__ __forceinline__ float swap16_add(float a, float b) {
  auto r = __builtin_amdgcn_permlane16_swap(asu(a), asu(b), false, false);
  const unsigned x = r[0], y = r[1];
  return asf(x) + asf(y);
}
__device__ __forceinline__ float swap32_max(float a) {
  auto r = __builtin_amdgcn_permlane32_swap(asu(a), asu(a), false, false);
  const unsigned x = r[0], y = r[1];
  return fmaxf(asf(x), asf(y));
}
__device__ __forceinline__ float swap16_max(float a) {
  auto r = __builtin_amdgcn_permlane16_swap(asu(a), asu(a), false, false);
  const unsigned x = r[0], y = r[1];
  return fmaxf(asf(x), asf(y));
}

__device__ __forceinline__ float reduce8(const float (&v)[8], int lane) {
  float a0 = swap32_add(v[0], v[4]), a1 = swap32_add(v[1], v[5]), a2 = swap32_add(v[2], v[6]), a3 = swap32_add(v[3], v[7]);
  float b0 = swap16_add(a0, a2), b1 = swap16_add(a1, a3);
  const bool h3 = (lane & 8) != 0;
  float keep = h3 ? b1 : b0, give = h3 ? b0 : b1;
  float c = keep + dpp<0x128>(give);
  c += dpp<0x141>(c); c += dpp<0x4E>(c); c += dpp<0xB1>(c);
  return c;
}
__device__ __forceinline__ v2u trread(LAS unsigned char* p) {
  return __builtin_bit_cast(v2u, __builtin_amdgcn_ds_read_tr16_b64_v4i16((LAS s16x4*)p));
}


__device__ __forceinline__ void wait_clr(rsrc_t rs, int blk, u64 tag) {
  POLL {
    const v2u f = ld64(rs, OFF_CLR + blk * 128);
    if ((f.x == (unsigned)tag) & (f.y == (unsigned)(tag >> 32))) break;
    __builtin_amdgcn_s_sleep(2);
    MEMBAR();
  }
}
template <class T> __device__ __forceinline__ T pin(T x) { asm volatile("" : "+v"(x)); return x; }
#define LP(T, off) ((LAS T*)(L + (off)))
#define SUM8(k) (((r[2 * (k)][0] + r[2 * (k)][1]) + (r[2 * (k)][2] + r[2 * (k)][3])) + ((r[2 * (k) + 1][0] + r[2 * (k) + 1][1]) + (r[2 * (k) + 1][2] + r[2 * (k) + 1][3])))
#define OH(d) v4u{(d) & mk[0], (d) & mk[1], (d) & mk[2], (d) & mk[3]}
__device__ __forceinline__ unsigned umax(unsigned a, unsigned b) { return a > b ? a : b; }
__device__ __forceinline__ unsigned mx4(v4u v) { return umax(umax(v.x, v.y), umax(v.z, v.w)); }

__device__ __forceinline__ void ln_stats(float v, LAS float* scr, int w, int lane, float& mb, float& rden) {
  float s1 = v, s2 = v * v;
  s1 += dpp<0xB1>(s1); s2 += dpp<0xB1>(s2); s1 += dpp<0x4E>(s1); s2 += dpp<0x4E>(s2);
  s1 += dpp<0x141>(s1); s2 += dpp<0x141>(s2); s1 += dpp<0x140>(s1); s2 += dpp<0x140>(s2);
  float t = swap16_add(s1, s2);
  t = swap32_add(t, t);
  if ((lane & 47) == 0) scr[(lane >> 4) * 8 + w] = t;
  __syncthreads();
  const f32x4 a0 = *(const LAS f32x4*)scr, a1 = *(const LAS f32x4*)(scr + 4), q0 = *(const LAS f32x4*)(scr + 8), q1 = *(const LAS f32x4*)(scr + 12);
  const float mean = (((a0[0] + a0[1]) + (a0[2] + a0[3])) + ((a1[0] + a1[1]) + (a1[2] + a1[3]))) * (1.f / 512.f);
  const float ex2 = (((q0[0] + q0[1]) + (q0[2] + q0[3])) + ((q1[0] + q1[1]) + (q1[2] + q1[3]))) * (1.f / 512.f);
  const float var = rbf(fmaxf(ex2 - mean * mean, 0.f));
  mb = rbf(mean);
  rden = __builtin_amdgcn_rcpf(rbf(__builtin_amdgcn_sqrtf(rbf(var + 1e-5f))));
}

__global__ void __launch_bounds__(NT, 2) fused_block(Params p) {
  extern __shared__ __attribute__((aligned(16))) unsigned char lds_raw[];
  LAS unsigned char* L = (LAS unsigned char*)lds_raw;
  const int tid = threadIdx.x, lane = tid & 63, w = __builtin_amdgcn_readfirstlane(tid >> 6);
  const int g = lane >> 4, j16 = lane & 15;
  const int b = blockIdx.x, xcd = b & 7, idx = b >> 3;
  const rsrc_t rs = __builtin_amdgcn_make_buffer_rsrc((void*)p.ws, 0, WS_BYTES, 0x00020000);
  unsigned pf[8] = {0u, 0u, 0u, 0u, 0u, 0u, 0u, 0u};
  if (idx >= 8 && idx < 10 && w == 7) {
    unsigned long long pc; asm volatile("s_getpc_b64 %0" : "=s"(pc));
    const GAS unsigned* cp = (const GAS unsigned*)(pc & ~1023ull) + lane * 4 + (idx - 8) * 2048;
#pragma unroll
    for (int i = 0; i < 8; ++i) pf[i] = cp[i * 256];
  }
  v4u buf[16];
  const int r1 = 128 * ((xcd * 8 + idx) >> 4 & 3) + 16 * w + (lane >> 3);
  const unsigned c0raw = p.c[r1], c1raw = p.c[r1 + 8];
  {
    const int hq = 4 * xcd + ((idx - 8) >> 1), a_ = idx - 16;
    const bf16_t* base = idx < 8 ? p.wmod : idx < 16 ? p.wq : p.ckv;
    const int offM = r1 * 3072 + 64 * ((xcd * 8 + idx) & 15) + 8 * (lane & 7);
    const int offQ = ((256 * (idx & 1) + 32 * w + 8 * g) * 32 + hq) * 128 + 8 * j16;
    const int offA = (128 * (4 * xcd + (a_ >> 2)) + 16 * ((w + 2 * (a_ & 3)) & 7)) * 512 + 8 * lane;
    const int stride = idx < 8 ? 8 * 3072 : idx < 16 ? 4096 : 512;
    const bf16_t* src = base + (idx < 8 ? offM : idx < 16 ? offQ : offA);
    buf[0] = ldw(src); buf[1] = ldw(src + stride);
    if (idx >= 8) {
#pragma unroll
      for (int i = 2; i < 8; ++i) buf[i] = ldw(src + i * stride);
      if (idx >= 16) {
#pragma unroll
        for (int i = 8; i < 16; ++i) buf[i] = ldw(src + i * 512);
      }
    }
  }
  const v2u ep = ld64(rs, OFF_EPOCH);
  __builtin_amdgcn_sched_barrier(0);
  const v4u sent4 = {SENT, SENT, SENT, SENT};
  const int q4 = j16 >> 2, p4 = j16 & 3;

  if (idx < 8) {
    const int midx = xcd * 8 + idx;
    const bool is7 = midx < 43, is8 = midx >= 56, isM = midx >= 43 && midx < 59;
    const unsigned ctid = p.c[tid];
    const int h = 4 * xcd + (idx >> 1), chf = idx & 1;
    const int cb1 = midx & 15, ks1 = midx >> 4;
    const v4u wA = buf[0], wB = buf[1];
    const unsigned xraw = p.x[tid];
    __builtin_amdgcn_sched_barrier(0);
    {
      const float cv0 = bf2f(c0raw), cv1 = bf2f(c1raw);
      const float s0 = rbf(cv0 * __builtin_amdgcn_rcpf(1.f + __expf(-cv0))), s1 = rbf(cv1 * __builtin_amdgcn_rcpf(1.f + __expf(-cv1)));
      const float a0 = swap32_add(s0 * bflo(wA.x) + s1 * bflo(wB.x), s0 * bflo(wA.z) + s1 * bflo(wB.z));
      const float a1 = swap32_add(s0 * bfhi(wA.x) + s1 * bfhi(wB.x), s0 * bfhi(wA.z) + s1 * bfhi(wB.z));
      const float a2 = swap32_add(s0 * bflo(wA.y) + s1 * bflo(wB.y), s0 * bflo(wA.w) + s1 * bflo(wB.w));
      const float a3 = swap32_add(s0 * bfhi(wA.y) + s1 * bfhi(wB.y), s0 * bfhi(wA.w) + s1 * bfhi(wB.w));
      const float b0 = swap16_add(a0, a2), b1 = swap16_add(a1, a3);
      const bool h3 = (lane & 8) != 0;
      const float c = (h3 ? b1 : b0) + dpp<0x128>(h3 ? b0 : b1);
      ((LAS float*)(L + L_RED))[(8 * (lane & 7) + (lane >> 3)) * 8 + w] = c;
    }
    __syncthreads();
    const u64 tag = (((u64)ep.y << 32) | ep.x) + 1ull;
    if (tid < 64) {
      const f32x4 s0 = *(const LAS f32x4*)(L + L_RED + tid * 32), s1 = *(const LAS f32x4*)(L + L_RED + tid * 32 + 16);
      const float sum = ((s0[0] + s0[1]) + (s0[2] + s0[3])) + ((s1[0] + s1[1]) + (s1[2] + s1[3]));
      st64(rs, OFF_G1 + (ks1 * 1024 + 64 * cb1 + tid) * 8, v2u{asu(sum), (unsigned)tag});
    }
    __builtin_amdgcn_s_barrier();
    if (w == 1) {
      st128(rs, OFF_QA + (h * 2 + chf) * 1024 + lane * 16, sent4);
      if (isM && lane < 32) st128(rs, OFF_MOD + (128 * (midx - 43) + 4 * lane) * 4, sent4);
      if (is7) {
        st128(rs, OFF_OUTP + midx * 2048 + lane * 16, sent4);
        st128(rs, OFF_OUTP + midx * 2048 + 1024 + lane * 16, sent4);
      }
    }
    v4u wb[8];
    {
      const bf16_t* src2 = p.wuk + ((size_t)(256 * chf + 32 * w + g) * 32 + h) * 128 + 8 * j16;
#pragma unroll
      for (int i = 0; i < 8; ++i) wb[i] = ldw(src2 + (size_t)(4 * i) * 4096);
    }
    __builtin_amdgcn_sched_barrier(0);
    if (w == 1) {
      asm volatile("s_waitcnt vmcnt(8)" ::: "memory");
      if (lane == 0) st64(rs, OFF_CLR + b * 128, v2u{(unsigned)tag, (unsigned)(tag >> 32)});
    }
    {
      LAS unsigned char* T = L + w * TR_WAVE;
#pragma unroll
      for (int i = 0; i < 8; ++i) *(LAS v4u*)(T + (4 * i + g) * 272 + 16 * j16) = wb[i];
#pragma unroll
      for (int t = 0; t < 2; ++t)
#pragma unroll
        for (int s = 0; s < 4; ++s) wb[4 * t + s] = *(const LAS v4u*)(T + (16 * t + j16) * 272 + 16 * (4 * s + g));
    }
    {
      const int aW = pin(L_RED2 + tid * 2), aR = pin(L_RED2 + 16 * g), aP = pin(L_P + (j16 & 1) * 512 + (32 * w + 8 * (j16 & 2) + 4 * g) * 2);
      const int aO = pin(L_P + lane * 16), oQ = pin(OFF_QA + (h * 2 + chf) * 1024 + lane * 16), oI = pin(OFF_QP + (h * 256 + tid) * 8);
      if (tid < 128) {
        v2u qa, qb;
        POLL {
          qa = ld64(rs, oI); qb = ld64(rs, oI + 1024);
          if (((qa.y ^ (unsigned)tag) | (qb.y ^ (unsigned)tag)) == 0u) break;
          MEMBAR();
        }
        const unsigned qp0 = qa.x, qp1 = qb.x;
        *LP(bf16_t, aW) = (bf16_t)f2bf(asf(qp0) + asf(qp1));
      }
      __syncthreads();
      v4u qf[4];
#pragma unroll
      for (int s = 0; s < 4; ++s) qf[s] = *LP(v4u, aR + 64 * s);
      f32x4 d0 = {0.f, 0.f, 0.f, 0.f}, d1 = {0.f, 0.f, 0.f, 0.f};
#pragma unroll
      for (int s = 0; s < 4; ++s) { d0 = mfma16(wb[s], qf[s], d0); d1 = mfma16(wb[4 + s], qf[s], d1); }
      if (j16 < 4) {
        const f32x4 d = (j16 & 2) ? d1 : d0;
        unsigned e[4];
#pragma unroll
        for (int r = 0; r < 4; ++r) { const float hi = rbf(d[r]); e[r] = f2bf((j16 & 1) ? d[r] - hi : hi); }
        *LP(v2u, aP) = v2u{san(e[0] | (e[1] << 16)), san(e[2] | (e[3] << 16))};
      }
      __syncthreads();
      if (w == 0) st128(rs, oQ, *LP(v4u, aO));
    }
    __builtin_amdgcn_s_barrier();
    v4u wl[8];
    if (is7) {
#pragma unroll
      for (int i = 0; i < 4; ++i) wl[i] = ldw(p.w1 + (size_t)(64 * w + 16 * i + (lane >> 2)) * 1376 + 32 * midx + 8 * (lane & 3));
#pragma unroll
      for (int i = 0; i < 4; ++i) wl[4 + i] = ldw(p.w2 + (size_t)(32 * midx + 8 * i + (lane >> 3)) * 512 + 64 * w + 8 * (lane & 7));
    }
    if (isM) {
      const bf16_t* src = p.wmod + (64 * w + 16 * g) * 3072 + 1024 + 128 * (midx - 43) + 8 * j16;
#pragma unroll
      for (int i = 0; i < 16; ++i) buf[i] = ldw(src + i * 3072);
      __builtin_amdgcn_sched_barrier(0);
      const float cv = bf2f(ctid);
      const unsigned sb_ = f2bf(cv * __builtin_amdgcn_rcpf(1.f + __expf(-cv)));
      ((LAS unsigned*)(L + L_RED2))[tid] = sb_ | (sb_ << 16);
      unsigned mk[4];
#pragma unroll
      for (int r = 0; r < 4; ++r) mk[r] = (j16 < 8 && (j16 >> 1) == r) ? ((j16 & 1) ? 0xFFFF0000u : 0x0000FFFFu) : 0u;
      f32x4 q0 = {0.f, 0.f, 0.f, 0.f}, q1 = {0.f, 0.f, 0.f, 0.f};
#pragma unroll
      for (int i4 = 0; i4 < 4; ++i4) {
        const v4u d = *(const LAS v4u*)(L + L_RED2 + (64 * w + 16 * g + 4 * i4) * 4);
        q0 = mfma16(OH(d.x), buf[4 * i4], q0); q1 = mfma16(OH(d.y), buf[4 * i4 + 1], q1);
        q0 = mfma16(OH(d.z), buf[4 * i4 + 2], q0); q1 = mfma16(OH(d.w), buf[4 * i4 + 3], q1);
      }
      q0 += q1;
      if (g < 2) *(LAS f32x4*)(L + (w * 128 + 8 * j16 + 4 * g) * 4) = q0;
      __syncthreads();
      if (tid < 128) {
        const LAS float* r = (const LAS float*)L + tid;
        float s0 = 0.f, s1 = 0.f;
#pragma unroll
        for (int i = 0; i < 8; i += 2) { s0 += r[i * 128]; s1 += r[(i + 1) * 128]; }
        st32(rs, OFF_MOD + (128 * (midx - 43) + tid) * 4, sanf(rbf(s0 + s1)));
      }
    }
    __builtin_amdgcn_sched_barrier(0);
    if (tid < 256) wait_clr(rs, tid, tag);
    const float xv = bf2f(xraw);
    {
      LAS unsigned char* T = L + w * TR_WAVE;
      if (is7) {
#pragma unroll
        for (int i = 0; i < 4; ++i) *(LAS v4u*)(T + (16 * i + (lane >> 2)) * TR_STRIDE + 16 * (lane & 3)) = wl[i];
#pragma unroll
        for (int i = 0; i < 4; ++i) *(LAS v4u*)(T + 8192 + (8 * i + (lane >> 3)) * 144 + 16 * (lane & 7)) = wl[4 + i];
#pragma unroll
        for (int t = 0; t < 2; ++t)
#pragma unroll
          for (int ks = 0; ks < 2; ++ks) {
            const int r0 = 32 * ks + 8 * g + q4, cb = (16 * t + 4 * p4) * 2;
            const v2u l2 = trread(T + r0 * TR_STRIDE + cb), h2 = trread(T + (r0 + 4) * TR_STRIDE + cb);
            wl[t * 2 + ks] = v4u{l2.x, l2.y, h2.x, h2.y};
          }
#pragma unroll
        for (int t = 0; t < 4; ++t) {
          const int r0 = 8 * g + q4, cb = (16 * t + 4 * p4) * 2;
          const v2u l2 = trread(T + 8192 + r0 * 144 + cb), h2 = trread(T + 8192 + (r0 + 4) * 144 + cb);
          wl[4 + t] = v4u{l2.x, l2.y, h2.x, h2.y};
        }
      }
    }
    __syncthreads();
    if (b == 0 && tid == 0) st64(rs, OFF_EPOCH, v2u{(unsigned)tag, (unsigned)(tag >> 32)});
    float gav = 0.f, sbv = 0.f, cb1v = 0.f, b1v = 0.f;
    const int mc = 64 * (midx - 56) + lane;
    if (is7) {
      unsigned mu[3];
      POLL {
#pragma unroll
        for (int i = 0; i < 3; ++i) mu[i] = ld32(rs, OFF_MOD + (i * 512 + tid) * 4);
        if (umax(umax(mu[0], mu[1]), mu[2]) != SENT) break;
        __builtin_amdgcn_s_sleep(2);
        MEMBAR();
      }
      b1v = bf2f(p.b1[32 * midx + (tid & 31)]);
      gav = asf(mu[0]); sbv = asf(mu[1]); cb1v = rbf(1.f + asf(mu[2]));
      {
        float v[8] = {cb1v, cb1v * cb1v, sbv, sbv * sbv, cb1v * sbv, 0.f, 0.f, 0.f};
        const float r = reduce8(v, lane);
        if ((lane & 7) == 0) ((LAS float*)(L + L_RED + 512))[(lane >> 3) * 8 + w] = r;
      }
    } else if (is8 && w == 0) {
      unsigned mu0, mu1;
      POLL {
        mu0 = ld32(rs, OFF_MOD + mc * 4); mu1 = ld32(rs, OFF_MOD + (1536 + mc) * 4);
        if (umax(mu0, mu1) != SENT) break;
        __builtin_amdgcn_s_sleep(2);
        MEMBAR();
      }
      gav = asf(mu0); sbv = asf(mu1);
      cb1v = bf2f(p.x[mc]); b1v = bf2f(p.b2[mc]);
    }
    __syncthreads();
    float sC = 0.f, sC2 = 0.f, sS = 0.f, sS2 = 0.f, sCS = 0.f;
    if (is7) {
      const LAS f32x4* r = (const LAS f32x4*)(L + L_RED + 512);
      sC = SUM8(0); sC2 = SUM8(1); sS = SUM8(2); sS2 = SUM8(3); sCS = SUM8(4);
    }
    if (is7) {
      const int aS = pin(L_RED + ((lane >> 3) * 8 + w) * 4), aT = pin(L_RED + lane * 4), aY = pin(L_RED2 + w * 128 + lane * 2), aYr = pin(L_RED2 + w * 128 + 16 * g);
      const int aD = pin(L_P + 2048 + ((16 * j16 + 4 * g) * 8 + w) * 4), aG = pin(L_P + 2048 + (lane & 31) * 32);
      const int oO = pin(OFF_OUTP + (midx * 512 + 64 * w + 16 * j16 + 4 * g) * 4), oA = pin(OFF_AOP + tid * 4);
      gav = pin(gav); sbv = pin(sbv); cb1v = pin(cb1v); b1v = pin(b1v);
      sC = pin(sC); sC2 = pin(sC2); sS = pin(sS); sS2 = pin(sS2); sCS = pin(sCS);
      const float xvp = pin(xv);
      unsigned ap[8];
      POLL {
#pragma unroll
        for (int i = 0; i < 8; ++i) ap[i] = ld32(rs, oA + i * 2048);
        if (umax(umax(umax(ap[0], ap[1]), umax(ap[2], ap[3])), umax(umax(ap[4], ap[5]), umax(ap[6], ap[7]))) != SENT) break;
        MEMBAR();
      }
      const float ao = ((asf(ap[0]) + asf(ap[1])) + (asf(ap[2]) + asf(ap[3]))) + ((asf(ap[4]) + asf(ap[5])) + (asf(ap[6]) + asf(ap[7])));
      const float x1 = rbf(xvp + rbf(gav * rbf(ao)));
      {
        const float xc = x1 * cb1v;
        float v[8] = {x1, x1 * x1, xc, xc * xc, xc * cb1v, xc * sbv, 0.f, 0.f};
        const float r = reduce8(v, lane);
        if ((lane & 7) == 0) *LP(float, aS) = r;
      }
      __syncthreads();
      float y;
      {
        float t = *LP(float, aT);
        t += dpp<0xB1>(t); t += dpp<0x4E>(t); t += dpp<0x141>(t);
#define RL(k) asf((unsigned)__builtin_amdgcn_readlane((int)asu(t), 8 * (k)))
        const float sX = RL(0), sX2 = RL(1), sXC = RL(2), sX2C2 = RL(3), sXC2 = RL(4), sXCS = RL(5);
        const float mean1 = sX * (1.f / 512.f);
        const float var1 = rbf(fmaxf(sX2 * (1.f / 512.f) - mean1 * mean1, 0.f));
        const float mb1 = rbf(mean1);
        const float rd1 = __builtin_amdgcn_rcpf(rbf(__builtin_amdgcn_sqrtf(rbf(var1 + 1e-5f))));
        const float h2 = rbf(rbf(rbf(rbf(x1 - mb1) * rd1) * cb1v) + sbv);
        const float sU = (sXC - mb1 * sC) * rd1;
        const float sU2 = (sX2C2 - 2.f * mb1 * sXC2 + mb1 * mb1 * sC2) * rd1 * rd1;
        const float sUS = (sXCS - mb1 * sCS) * rd1;
        const float mean2 = (sU + sS) * (1.f / 512.f);
        const float var2 = rbf(fmaxf((sU2 + 2.f * sUS + sS2) * (1.f / 512.f) - mean2 * mean2, 0.f));
        const float rd2 = __builtin_amdgcn_rcpf(rbf(__builtin_amdgcn_sqrtf(rbf(var2 + 1e-5f))));
        y = rbf(h2 - rbf(mean2)) * rd2;
      }
      *LP(bf16_t, aY) = (bf16_t)f2bf(y);
      const v4u y0 = *LP(v4u, aYr), y1 = *LP(v4u, aYr + 64);
      f32x4 d0 = {0.f, 0.f, 0.f, 0.f}, d1 = {0.f, 0.f, 0.f, 0.f};
      d0 = mfma16(wl[0], y0, d0); d1 = mfma16(wl[2], y0, d1);
      d0 = mfma16(wl[1], y1, d0); d1 = mfma16(wl[3], y1, d1);
      if (j16 < 2) {
        const f32x4 d = j16 ? d1 : d0;
        LAS float* o = LP(float, aD);
        o[0] = d[0]; o[8] = d[1]; o[16] = d[2]; o[24] = d[3];
      }
      __syncthreads();
      {
        const LAS f32x4* r = LP(f32x4, aG);
        const float s = rbf(rbf(SUM8(0)) + b1v);
        const float u = 0.7978845608028654f * (s + 0.044715f * s * s * s);
        *LP(bf16_t, aY) = (bf16_t)f2bf(s * __builtin_amdgcn_rcpf(1.f + __expf(-2.f * u)));
        const v4u hb = *LP(v4u, aYr);
        const f32x4 z = {0.f, 0.f, 0.f, 0.f};
        const f32x4 e0 = mfma16(wl[4], hb, z), e1 = mfma16(wl[5], hb, z), e2 = mfma16(wl[6], hb, z), e3 = mfma16(wl[7], hb, z);
        const f32x4 e = j16 == 0 ? e0 : j16 == 1 ? e1 : j16 == 2 ? e2 : e3;
        if (j16 < 4) st128(rs, oO, v4u{sanf(e[0]), sanf(e[1]), sanf(e[2]), sanf(e[3])});
      }
    } else if (is8) {
      {
        unsigned u2;
        POLL { u2 = ld32(rs, OFF_AOP + (w * 512 + mc) * 4); if (u2 != SENT) break; MEMBAR(); }
        ((LAS float*)(L + L_RED))[lane * 8 + w] = asf(u2);
      }
      __syncthreads();
      float x1 = 0.f;
      if (w == 0) {
        const LAS f32x4* r = (const LAS f32x4*)(L + L_RED + lane * 32);
        x1 = rbf(cb1v + rbf(gav * rbf(SUM8(0))));
      }
      x1 = pin(x1); sbv = pin(sbv); b1v = pin(b1v);
      const int aW = pin(L_P + 2048 + (lane * 8 + w) * 4), aR = pin(L_P + 2048 + lane * 32);
      const int oH = pin(OFF_OUTP + (w * 512 + mc) * 4), oH5 = pin(OFF_OUTP + ((w < 3 ? w + 40 : w) * 512 + mc) * 4);
      GAS bf16_t* op = (GAS bf16_t*)(p.out + mc);
      asm volatile("" : "+v"(op));
      unsigned hv[6];
      POLL {
#pragma unroll
        for (int i = 0; i < 5; ++i) hv[i] = ld32(rs, oH + i * 16384);
        hv[5] = ld32(rs, oH5);
        if (umax(umax(umax(hv[0], hv[1]), umax(hv[2], hv[3])), umax(hv[4], hv[5])) != SENT) break;
        MEMBAR();
      }
      float s = ((asf(hv[0]) + asf(hv[1])) + (asf(hv[2]) + asf(hv[3]))) + asf(hv[4]);
      if (w < 3) s += asf(hv[5]);
      *LP(float, aW) = s;
      __syncthreads();
      if (w == 0) {
        const LAS f32x4* r = LP(f32x4, aR);
        *op = (bf16_t)f2bf(x1 + rbf(sbv * rbf(rbf(SUM8(0)) + b1v)));
      }
    }
  } else if (idx >= 16) {
    const int a = idx - 16, kc = 4 * xcd + (a >> 2), hg = a & 3;
    const int wr = (w + 2 * hg) & 7;
    v4u (&kv)[16] = buf;
    st128(rs, OFF_PART + ((kc * 4 + hg) * 8 + w) * 1024 + lane * 16, sent4);
    if (w == 0 && lane < 8) st128(rs, OFF_STAT + (kc * 4 + hg) * 128 + lane * 16, sent4);
    if (w == 2 && lane < 8) st128(rs, OFF_AOP + (xcd * 512 + 32 * a + 4 * lane) * 4, sent4);
    __builtin_amdgcn_sched_barrier(0);
    const u64 tag = (((u64)ep.y << 32) | ep.x) + 1ull;
#pragma unroll
    for (int s = 0; s < 16; ++s) *(LAS v4u*)(L + L_KV + (16 * wr + s) * KV_STRIDE + lane * 16) = kv[s];
    __syncthreads();
#pragma unroll
    for (int s = 0; s < 16; ++s) kv[s] = *(const LAS v4u*)(L + L_KV + (16 * w + j16) * KV_STRIDE + (32 * s + 8 * g) * 2);
    v4u vt[16];
#pragma unroll
    for (int t = 0; t < 4; ++t)
#pragma unroll
      for (int ks = 0; ks < 4; ++ks) {
        const int r0 = 32 * ks + 4 * g + q4;
        const int cb = (64 * w + 16 * t + 4 * p4) * 2;
        const v2u l2 = trread(L + L_KV + r0 * KV_STRIDE + cb), h2 = trread(L + L_KV + (r0 + 16) * KV_STRIDE + cb);
        vt[t * 4 + ks] = v4u{l2.x, l2.y, h2.x, h2.y};
      }
    if (tid < 16) wait_clr(rs, ((tid & 7) << 3) + 2 * hg + (tid >> 3), tag);
    asm volatile("s_waitcnt vmcnt(0)" ::: "memory");
    __syncthreads();
    if (tid == 0) st64(rs, OFF_CLR + b * 128, v2u{(unsigned)tag, (unsigned)(tag >> 32)});
    const bool lo_half = j16 >= 8;
    const int hd = j16 & 7;
    {
      const int hq = tid >> 7, sq = 8 * ((tid >> 6) & 1) + ((tid >> 2) & 7), hl = (tid >> 5) & 1, gq = tid & 3;
      const int aQ0 = pin((sq * 64 + gq * 16 + ((hl * 8 + hq) ^ gq)) * 16), aQ1 = pin((sq * 64 + gq * 16 + ((hl * 8 + hq + 4) ^ gq)) * 16);
      const int aB = pin((lane ^ g) * 16), aMw = pin(L_RED + (lane * 8 + w) * 4), aMr = pin(L_RED + hd * 32);
      const int aPw = pin(L_P + j16 * P_STRIDE + (32 * (w >> 1) + 8 * g + 4 * (w & 1)) * 2), aPr = pin(L_P + j16 * P_STRIDE + 16 * g);
      const int oP = pin(OFF_PART + ((kc * 4 + hg) * 8 + w) * 1024 + (hd * 2 + (lo_half ? 1 : 0)) * 64 + g * 16);
      const int oS = pin(OFF_STAT + (kc * 4 + hg) * 128 + lane * 8), o0 = pin(OFF_QA + hg * 16384 + tid * 16);
      v4u q0, q1;
      POLL {
        q0 = ld128(rs, o0); q1 = ld128(rs, o0 + 8192);
        if (umax(mx4(q0), mx4(q1)) != SENT) break;
        MEMBAR();
      }
      *LP(v4u, aQ0) = q0;
      *LP(v4u, aQ1) = q1;
      __syncthreads();
      v4u bq[16];
#pragma unroll
      for (int s = 0; s < 16; ++s) bq[s] = *LP(v4u, aB + s * 1024);
      __builtin_amdgcn_sched_barrier(0);
      f32x4 s0 = {0.f, 0.f, 0.f, 0.f}, s1 = {0.f, 0.f, 0.f, 0.f};
#pragma unroll
      for (int s = 0; s < 16; s += 2) { s0 = mfma16(kv[s], bq[s], s0); s1 = mfma16(kv[s + 1], bq[s + 1], s1); }
      float o[4];
#pragma unroll
      for (int r = 0; r < 4; ++r) {
        float v = s0[r] + s1[r];
        v += dpp<0x128>(v);
        o[r] = rbf(rbf(v) * 0.08838834764831845f);
      }
      float m = fmaxf(fmaxf(o[0], o[1]), fmaxf(o[2], o[3]));
      m = swap16_max(m); m = swap32_max(m);
      if (lane < 8) *LP(float, aMw) = m;
      __syncthreads();
      const f32x4 m0v = *LP(f32x4, aMr), m1v = *LP(f32x4, aMr + 16);
      const float M = fmaxf(fmaxf(fmaxf(m0v[0], m0v[1]), fmaxf(m0v[2], m0v[3])), fmaxf(fmaxf(m1v[0], m1v[1]), fmaxf(m1v[2], m1v[3])));
      float e[4], x[4];
#pragma unroll
      for (int r = 0; r < 4; ++r) {
        e[r] = __expf(o[r] - M);
        const float hi = rbf(e[r]);
        x[r] = lo_half ? e[r] - hi : hi;
      }
      *LP(v2u, aPw) = v2u{f2bf(x[0]) | (f2bf(x[1]) << 16), f2bf(x[2]) | (f2bf(x[3]) << 16)};
      float l = (e[0] + e[1]) + (e[2] + e[3]);
      l = swap16_add(l, l); l = swap32_add(l, l);
      if (lane < 8) *LP(float, aMw + 2048) = l;
      __syncthreads();
      v4u pa[4];
#pragma unroll
      for (int ks = 0; ks < 4; ++ks) pa[ks] = *LP(v4u, aPr + 64 * ks);
      f32x4 acc[4];
#pragma unroll
      for (int t = 0; t < 4; ++t) acc[t] = f32x4{0.f, 0.f, 0.f, 0.f};
#pragma unroll
      for (int ks = 0; ks < 4; ++ks)
#pragma unroll
        for (int t = 0; t < 4; ++t) acc[t] = mfma16(vt[t * 4 + ks], pa[ks], acc[t]);
      if (w == 7 && lane < 8) {
        const LAS f32x4* r = LP(f32x4, aMr + 2048);
        st64(rs, oS, v2u{sanf(M), sanf(SUM8(0))});
      }
      unsigned ow[4];
#pragma unroll
      for (int u = 0; u < 2; ++u) {
        float v[4];
#pragma unroll
        for (int r = 0; r < 4; ++r) {
          const float keep = lo_half ? acc[2 + u][r] : acc[u][r], give = lo_half ? acc[u][r] : acc[2 + u][r];
          v[r] = keep + dpp<0x128>(give);
        }
        ow[2 * u] = san(f2bf(v[0]) | (f2bf(v[1]) << 16));
        ow[2 * u + 1] = san(f2bf(v[2]) | (f2bf(v[3]) << 16));
      }
      st128(rs, oP, v4u{ow[0], ow[1], ow[2], ow[3]});
    }
    __builtin_amdgcn_s_barrier();
    {
      v4u wo[4];
      const bf16_t* src2 = p.wo + (size_t)(512 * xcd + 64 * w + (lane >> 2)) * 512 + 32 * a + 8 * (lane & 3);
#pragma unroll
      for (int i = 0; i < 4; ++i) wo[i] = ldw(src2 + (size_t)(16 * i) * 512);
      LAS unsigned char* T = L + 32768 + w * 8192;
#pragma unroll
      for (int i = 0; i < 4; ++i) *(LAS v4u*)(T + (16 * i + (lane >> 2)) * TR_STRIDE + 16 * (lane & 3)) = wo[i];
#pragma unroll
      for (int t = 0; t < 2; ++t)
#pragma unroll
        for (int ks = 0; ks < 2; ++ks) {
          const int r0 = 32 * ks + 8 * g + q4, cb = (16 * t + 4 * p4) * 2;
          const v2u l2 = trread(T + r0 * TR_STRIDE + cb), h2 = trread(T + (r0 + 4) * TR_STRIDE + cb);
          wo[t * 2 + ks] = v4u{l2.x, l2.y, h2.x, h2.y};
        }
      if (lane < 2) wait_clr(rs, ((8 + 2 * (w >> 1) + lane) << 3) + xcd, tag);
      const int aY = pin(w * 128 + lane * 2), aYr = pin(w * 128 + 16 * g), aD = pin(2048 + ((16 * j16 + 4 * g) * 8 + w) * 4), aR = pin(2048 + tid * 32);
      const int o0 = pin(OFF_CTXP + ((4 * xcd + (tid >> 7)) * 256 + (tid & 127)) * 4), oA = pin(OFF_AOP + (xcd * 512 + 32 * a + tid) * 4);
      unsigned cp0, cp1;
      POLL {
        cp0 = ld32(rs, o0); cp1 = ld32(rs, o0 + 512);
        if (umax(cp0, cp1) != SENT) break;
        MEMBAR();
      }
      *LP(bf16_t, aY) = (bf16_t)f2bf(asf(cp0) + asf(cp1));
      const v4u y0 = *LP(v4u, aYr), y1 = *LP(v4u, aYr + 64);
      f32x4 d0 = {0.f, 0.f, 0.f, 0.f}, d1 = {0.f, 0.f, 0.f, 0.f};
      d0 = mfma16(wo[0], y0, d0); d1 = mfma16(wo[2], y0, d1);
      d0 = mfma16(wo[1], y1, d0); d1 = mfma16(wo[3], y1, d1);
      if (j16 < 2) {
        const f32x4 d = j16 ? d1 : d0;
        LAS float* o = LP(float, aD);
        o[0] = d[0]; o[8] = d[1]; o[16] = d[2]; o[24] = d[3];
      }
      __syncthreads();
      if (tid < 32) {
        const LAS f32x4* r = LP(f32x4, aR);
        st32(rs, oA, sanf(SUM8(0)));
      }
    }
  } else {
    constexpr int L_QP = 0, L_QB = 4096, L_CY = 8192;
    const int qi = idx - 8, qidx = xcd * 8 + qi;
    const int h = 4 * xcd + (qi >> 1), jq = qi & 1;
    unsigned mk[4];
#pragma unroll
    for (int r = 0; r < 4; ++r) mk[r] = ((j16 & 7) >> 1) == r ? ((j16 & 1) ? 0xFFFF0000u : 0x0000FFFFu) : 0u;
    v4u (&wa)[16] = buf;
    const int mq = 256 * jq + 32 * w + 2 * (lane & 7) + ((lane >> 4) & 1);
    const unsigned xt = p.x[tid], xm0 = p.x[mq], xm1 = p.x[mq + 16];
    __builtin_amdgcn_sched_barrier(0);
    const u64 tag = (((u64)ep.y << 32) | ep.x) + 1ull;
    float mbx, rdx;
    ln_stats(bf2f(xt), (LAS float*)(L + L_QB + 1024), w, lane, mbx, rdx);
    asm volatile("" :: "v"(pf[0] ^ pf[1] ^ pf[2] ^ pf[3] ^ pf[4] ^ pf[5] ^ pf[6] ^ pf[7]));
    const float nx = pin(rbf(rbf(bf2f(lane < 32 ? xm0 : xm1) - mbx) * rdx));
#pragma unroll
    for (int r = 0; r < 4; ++r) mk[r] = pin(mk[r]);
    {
      unsigned mz[4];
#pragma unroll
      for (int r = 0; r < 4; ++r) mz[r] = pin(j16 < 8 ? mk[r] : 0u);
#define OZ(d) v4u{(d) & mz[0], (d) & mz[1], (d) & mz[2], (d) & mz[3]}
      const int aH = pin(L_QB + 2048 + w * 128 + (16 * (lane >> 5) + 2 * (lane & 7) + ((lane >> 4) & 1)) * 4), aHr = pin(L_QB + 2048 + w * 128 + 32 * g);
      const int aQ = pin(L_QP + (w * 128 + 8 * j16 + 4 * g) * 4), aR = pin(L_QP + tid * 4), oQ = pin(OFF_QP + ((h * 2 + jq) * 128 + tid) * 8);
      const int o0 = pin(OFF_G1 + (((lane >> 3) & 3) * 1024 + (lane >> 5) * 512 + 256 * jq + 32 * w + 2 * (lane & 7)) * 8);
      v4u g0, g1;
      POLL {
        g0 = ld128(rs, o0); g1 = ld128(rs, o0 + 128);
        if ((((g0.y ^ (unsigned)tag) | (g0.w ^ (unsigned)tag)) | ((g1.y ^ (unsigned)tag) | (g1.w ^ (unsigned)tag))) == 0u) break;
        MEMBAR();
      }
      float v0 = swap16_add(asf(g0.x), asf(g0.z)), v1 = swap16_add(asf(g1.x), asf(g1.z));
      v0 += dpp<0x128>(v0); v1 += dpp<0x128>(v1);
      auto sw0 = __builtin_amdgcn_permlane32_swap(asu(rbf(v0)), asu(rbf(v1)), false, false);
      const unsigned sa_u = sw0[0], ca_u = sw0[1];
      const unsigned hb = f2bf(rbf(nx * rbf(1.f + asf(ca_u))) + asf(sa_u));
      *LP(unsigned, aH) = hb | (hb << 16);
      const v4u d0 = *LP(v4u, aHr), d1 = *LP(v4u, aHr + 16);
      f32x4 q0 = {0.f, 0.f, 0.f, 0.f}, q1 = {0.f, 0.f, 0.f, 0.f};
      q0 = mfma16(OZ(d0.x), wa[0], q0); q1 = mfma16(OZ(d0.y), wa[1], q1);
      q0 = mfma16(OZ(d0.z), wa[2], q0); q1 = mfma16(OZ(d0.w), wa[3], q1);
      q0 = mfma16(OZ(d1.x), wa[4], q0); q1 = mfma16(OZ(d1.y), wa[5], q1);
      q0 = mfma16(OZ(d1.z), wa[6], q0); q1 = mfma16(OZ(d1.w), wa[7], q1);
      q0 += q1;
      if (g < 2) *LP(f32x4, aQ) = q0;
      __syncthreads();
      if (tid < 128) {
        const LAS float* r = LP(float, aR);
        float s0 = 0.f, s1 = 0.f;
#pragma unroll
        for (int i = 0; i < 8; i += 2) { s0 += r[i * 128]; s1 += r[(i + 1) * 128]; }
        st64(rs, oQ, v2u{asu(s0 + s1), (unsigned)tag});
      }
    }
    __builtin_amdgcn_s_barrier();
    if (w == 2) {
      if (lane < 32) st128(rs, OFF_CTXP + ((h * 2 + jq) * 128 + lane * 4) * 4, sent4);
      asm volatile("s_waitcnt vmcnt(0)" ::: "memory");
      if (lane == 0) st64(rs, OFF_CLR + b * 128, v2u{(unsigned)tag, (unsigned)(tag >> 32)});
    }
    __builtin_amdgcn_s_sleep(56);
    {
      const bf16_t* src = p.wuv + ((size_t)(256 * jq + 32 * w + 16 * (g >> 1) + 2 * (g & 1)) * 32 + h) * 128 + 8 * j16;
#pragma unroll
      for (int i = 0; i < 8; ++i) wa[i] = ldw(src + (size_t)(4 * (i >> 1) + (i & 1)) * 4096);
    }
    __builtin_amdgcn_sched_barrier(0);
    if (tid >= 64 && tid < 96) wait_clr(rs, ((16 + 4 * (tid & 3) + (h >> 3)) << 3) + ((tid - 64) >> 2), tag);
    __syncthreads();
    {
      v4u p0, p1;
      const int sl = lane >> 2;
      const int o0 = pin(OFF_PART + ((sl * 4 + (h >> 3)) * 8 + 4 * jq + (w >> 1)) * 1024 + ((h & 7) * 2 + (w & 1)) * 64 + (lane & 3) * 16);
      const int aC = pin(L_QB + 3072 + sl * 4), aCw = pin(L_QB + 3072 + lane * 4), aY = pin(L_CY + ((w * 2 + (g >> 1)) * 128 + 8 * j16 + 4 * (g & 1)) * 4);
      const int aR = pin(L_CY + tid * 4), oC = pin(OFF_CTXP + ((h * 2 + jq) * 128 + tid) * 4);
      if (w == 0) {
        v2u st;
        POLL {
          st = ld64(rs, OFF_STAT + ((lane & 31) * 4 + (h >> 3)) * 128 + (h & 7) * 8);
          p0 = ld128(rs, o0); p1 = ld128(rs, o0 + 16 * 32768);
          if (umax(umax(st.x, st.y), umax(mx4(p0), mx4(p1))) != SENT) break;
          MEMBAR();
        }
        const float ms = asf(st.x), ls = asf(st.y);
        float Mx = row_max(ms); Mx = swap16_max(Mx);
        const float e = __expf(ms - Mx);
        float Ls = row_sum(e * ls); Ls = swap16_add(Ls, Ls);
        *LP(float, aCw) = e * __builtin_amdgcn_rcpf(Ls);
      } else {
        POLL {
          p0 = ld128(rs, o0); p1 = ld128(rs, o0 + 16 * 32768);
          if (umax(mx4(p0), mx4(p1)) != SENT) break;
          MEMBAR();
        }
      }
      __syncthreads();
      const float c0 = *LP(float, aC), c1 = *LP(float, aC + 64);
      const float a0 = swap32_add(c0 * bflo(p0.x) + c1 * bflo(p1.x), c0 * bflo(p0.z) + c1 * bflo(p1.z));
      const float a1 = swap32_add(c0 * bfhi(p0.x) + c1 * bfhi(p1.x), c0 * bfhi(p0.z) + c1 * bfhi(p1.z));
      const float a2 = swap32_add(c0 * bflo(p0.y) + c1 * bflo(p1.y), c0 * bflo(p0.w) + c1 * bflo(p1.w));
      const float a3 = swap32_add(c0 * bfhi(p0.y) + c1 * bfhi(p1.y), c0 * bfhi(p0.w) + c1 * bfhi(p1.w));
      float e0 = swap16_add(a0, a2), e1 = swap16_add(a1, a3);
      e0 += dpp<0x124>(e0); e1 += dpp<0x124>(e1); e0 += dpp<0x128>(e0); e1 += dpp<0x128>(e1);
      const float h0 = rbf(e0), h1 = rbf(e1);
      const unsigned x0 = f2bf(j16 < 8 ? h0 : e0 - h0), x1 = f2bf(j16 < 8 ? h1 : e1 - h1);
      const unsigned u0 = x0 | (x0 << 16), u1 = x1 | (x1 << 16);
      f32x4 q0 = {0.f, 0.f, 0.f, 0.f}, q1 = {0.f, 0.f, 0.f, 0.f};
      { const unsigned d = dppu<0x00>(u0); q0 = mfma16(OH(d), wa[0], q0); }
      { const unsigned d = dppu<0x00>(u1); q1 = mfma16(OH(d), wa[1], q1); }
      { const unsigned d = dppu<0x55>(u0); q0 = mfma16(OH(d), wa[2], q0); }
      { const unsigned d = dppu<0x55>(u1); q1 = mfma16(OH(d), wa[3], q1); }
      { const unsigned d = dppu<0xAA>(u0); q0 = mfma16(OH(d), wa[4], q0); }
      { const unsigned d = dppu<0xAA>(u1); q1 = mfma16(OH(d), wa[5], q1); }
      { const unsigned d = dppu<0xFF>(u0); q0 = mfma16(OH(d), wa[6], q0); }
      { const unsigned d = dppu<0xFF>(u1); q1 = mfma16(OH(d), wa[7], q1); }
      q0 += q1;
      *LP(f32x4, aY) = q0;
      __syncthreads();
      if (tid < 128) {
        const LAS float* r = LP(float, aR);
        float s0 = 0.f, s1 = 0.f;
#pragma unroll
        for (int i = 0; i < 16; i += 2) { s0 += r[i * 128]; s1 += r[(i + 1) * 128]; }
        st32(rs, oC, sanf(s0 + s1));
      }
    }
  }
}

extern "C" void kernel_launch(void* const* d_in, const int* in_sizes, int n_in,
                              void* d_out, int out_size, void* d_ws, size_t ws_size,
                              hipStream_t stream) {
  Params p;
  p.x = (const bf16_t*)d_in[0]; p.ckv = (const bf16_t*)d_in[1]; p.wq = (const bf16_t*)d_in[2];
  p.wuk = (const bf16_t*)d_in[3]; p.wuv = (const bf16_t*)d_in[4]; p.wo = (const bf16_t*)d_in[5];
  p.w1 = (const bf16_t*)d_in[7]; p.b1 = (const bf16_t*)d_in[8]; p.w2 = (const bf16_t*)d_in[9];
  p.b2 = (const bf16_t*)d_in[10]; p.c = (const bf16_t*)d_in[11]; p.wmod = (const bf16_t*)d_in[12];
  p.out = (bf16_t*)d_out; p.ws = (unsigned char*)d_ws;
  (void)hipFuncSetAttribute((const void*)fused_block, hipFuncAttributeMaxDynamicSharedMemorySize, LDS_BYTES);
  fused_block<<<NWG, NT, LDS_BYTES, stream>>>(p);
}
```
